```python
import torch

def _softmax(x, axis=-1):
    return torch.softmax(x, dim=axis)
def _gelu(x):
    return torch.nn.functional.gelu(x, approximate="tanh")
def _silu(x):
    return torch.nn.functional.silu(x)
def _sigmoid(x):
    return torch.sigmoid(x)
def _layernorm(x, axis=-1, eps=1e-5):
    m = x.mean(dim=axis, keepdim=True)
    v = x.var(dim=axis, keepdim=True, unbiased=False)
    return (x - m) / torch.sqrt(v + eps)
def _scatter_add(tgt, idx, src, axis=0):
    return tgt.clone().index_add_(axis, idx, src)
def _segment_sum(src, seg, n):
    out = torch.zeros((n,) + src.shape[1:], dtype=src.dtype)
    return out.index_add_(0, seg, src)
def _pad(x, pw):
    flat = tuple(v for pair in reversed(pw) for v in pair)
    return torch.nn.functional.pad(x, flat)

kernel_name = 'standalone__gqa_softmax__near_ridge'

def setup_inputs():

    _w = (lambda a: (lambda t: t.to(torch.bfloat16) if t.is_floating_point() else t)(torch.as_tensor(a)))
    B, Hq, Hkv, Sq, Sk, D = 64, 32, 8, 256, 256, 128
    return {
        "q": _w((lambda *s: torch.randn(*s, dtype=torch.bfloat16) * 0.8)(B, Hq, Sq, D)),
        "k": _w((lambda *s: torch.randn(*s, dtype=torch.bfloat16) * 0.8)(B, Hkv, Sk, D)),
        "v": _w((lambda *s: torch.randn(*s, dtype=torch.bfloat16) * 0.05)(B, Hkv, Sk, D)),
    }

def reference(q, k, v):
    B, Hq, Sq, D = q.shape
    Hkv = k.shape[1]
    rep = Hq // Hkv
    k = (lambda x, n, axis: torch.repeat_interleave(x, n, dim=axis))(k, rep, axis=1)
    v = (lambda x, n, axis: torch.repeat_interleave(x, n, dim=axis))(v, rep, axis=1)
    scale = D ** -0.5
    scores = torch.einsum("bhqd,bhkd->bhqk", q, k) * scale
    p = _softmax(scores, axis=-1)
    return torch.einsum("bhqk,bhkd->bhqd", p, v)
```

```cpp
#include <hip/hip_runtime.h>
#include <hip/hip_bf16.h>

using bf16 = __hip_bfloat16;
using bf16x8 = __attribute__((ext_vector_type(8))) short;
using s16x4  = __attribute__((ext_vector_type(4))) short;
using f32x16 = __attribute__((ext_vector_type(16))) float;
using u32x4  = __attribute__((ext_vector_type(4))) unsigned;
using u32x2  = __attribute__((ext_vector_type(2))) unsigned;

constexpr int B_ = 64, HQ = 32, HKV = 8, SQ = 256, SK = 256, D = 128;
constexpr int NGRP = B_ * HKV;
constexpr int GROWS = (HQ / HKV) * SQ;
constexpr int LDS_BYTES = 2 * SK * D * 2;
constexpr int VOFF = SK * D * 2;

#define MFMA(a, b, c) __builtin_amdgcn_mfma_f32_32x32x16_bf16((a), (b), (c), 0, 0, 0)

__device__ __forceinline__ unsigned cvtpk(float lo, float hi) {
  unsigned r; asm volatile("v_cvt_pk_bf16_f32 %0, %1, %2" : "=v"(r) : "v"(lo), "v"(hi)); return r;
}
template <int OFF> __device__ __forceinline__ s16x4 tr_read(int vb) {
  s16x4 r; asm volatile("ds_read_b64_tr_b16 %0, %1 offset:%2" : "=&v"(r) : "v"(vb), "i"(OFF) : "memory"); return r;
}

__global__ __launch_bounds__(512, 1)
void gqa_attn_kernel(const bf16* __restrict__ Q, const bf16* __restrict__ K, const bf16* __restrict__ V, bf16* __restrict__ O) {
  extern __shared__ __attribute__((aligned(16))) char lds[];
  const int tid = threadIdx.x, w = tid >> 6, l = tid & 63, r32 = l & 31, hi = l >> 5;
  constexpr float C = 0.088388347648318440f * 1.4426950408889634f;

  for (int g = blockIdx.x; g < NGRP; g += gridDim.x) {
    const bf16* Kg = K + (size_t)g * SK * D;
    const bf16* Vg = V + (size_t)g * SK * D;
    {
      bf16x8 kr[8], vr[8];
#pragma unroll
      for (int i = 0; i < 8; ++i) {
        const int j = w * 8 + i;
        const int key = (j >> 1) * 8 + (l & 7), ch = (j & 1) * 8 + (l >> 3);
        kr[i] = *reinterpret_cast<const bf16x8*>(Kg + key * D + ch * 8);
        const int kb = j >> 1, cb = (j & 1) * 2 + (l >> 5), row = (l >> 2) & 7, c8 = l & 3;
        vr[i] = *reinterpret_cast<const bf16x8*>(Vg + (kb * 8 + row) * D + cb * 32 + c8 * 8);
      }
#pragma unroll
      for (int i = 0; i < 8; ++i) {
        const int j = w * 8 + i;
        const int key = (j >> 1) * 8 + (l & 7), ch = (j & 1) * 8 + (l >> 3);
        *reinterpret_cast<bf16x8*>(lds + ch * 4096 + key * 16) = kr[i];
        *reinterpret_cast<bf16x8*>(lds + VOFF + j * 1024 + l * 16) = vr[i];
      }
    }
    __syncthreads();

    const int kbase = hi * 4096 + r32 * 16;
    const int vbase = (int)(uintptr_t)lds + VOFF + (4 * hi + ((l >> 2) & 3)) * 64 + ((l >> 4) & 1) * 32 + (l & 3) * 8;

    for (int it = 0; it < 4; ++it) {
      const size_t row0 = (size_t)g * GROWS + (it * 8 + w) * 32;
      const bf16* Qw = Q + (row0 + r32) * D + hi * 8;
      bf16x8 q[8];
#pragma unroll
      for (int d0 = 0; d0 < 8; ++d0) q[d0] = *reinterpret_cast<const bf16x8*>(Qw + d0 * 16);
      f32x16 s[8];
#pragma unroll
      for (int kt = 0; kt < 8; ++kt) {
        s[kt] = f32x16{};
#pragma unroll
        for (int d0 = 0; d0 < 8; ++d0) {
          bf16x8 kf = *reinterpret_cast<const bf16x8*>(lds + kbase + d0 * 8192 + kt * 512);
          s[kt] = MFMA(kf, q[d0], s[kt]);
        }
      }
      float m = s[0][0];
#pragma unroll
      for (int kt = 0; kt < 8; ++kt)
#pragma unroll
        for (int r = 0; r < 16; ++r) m = fmaxf(m, s[kt][r]);
      { auto rr = __builtin_amdgcn_permlane32_swap(__float_as_uint(m), __float_as_uint(m), false, false);
        m = fmaxf(__uint_as_float(rr[0]), __uint_as_float(rr[1])); }
      const float mC = -m * C;
      float lsum = 0.f;
      bf16x8 pa[16];
#pragma unroll
      for (int kt = 0; kt < 8; ++kt) {
#pragma unroll
        for (int r = 0; r < 16; ++r) { float p = __builtin_amdgcn_exp2f(fmaf(s[kt][r], C, mC)); s[kt][r] = p; lsum += p; }
#pragma unroll
        for (int h2 = 0; h2 < 2; ++h2) {
          u32x4 wv = {cvtpk(s[kt][8 * h2 + 0], s[kt][8 * h2 + 1]), cvtpk(s[kt][8 * h2 + 2], s[kt][8 * h2 + 3]),
                      cvtpk(s[kt][8 * h2 + 4], s[kt][8 * h2 + 5]), cvtpk(s[kt][8 * h2 + 6], s[kt][8 * h2 + 7])};
          pa[kt * 2 + h2] = __builtin_bit_cast(bf16x8, wv);
        }
      }
      { auto rr = __builtin_amdgcn_permlane32_swap(__float_as_uint(lsum), __float_as_uint(lsum), false, false);
        lsum = __uint_as_float(rr[0]) + __uint_as_float(rr[1]); }
      const float rl = __builtin_amdgcn_rcpf(lsum);
      f32x16 o[4];
#pragma unroll
      for (int d0 = 0; d0 < 4; ++d0) o[d0] = f32x16{};
#define PVSTEP(ks) do { \
        const s16x4 a0 = tr_read<(2 * (ks)) * 2048 + 0 * 512>(vbase), a1 = tr_read<(2 * (ks) + 1) * 2048 + 0 * 512>(vbase); \
        const s16x4 b0 = tr_read<(2 * (ks)) * 2048 + 1 * 512>(vbase), b1 = tr_read<(2 * (ks) + 1) * 2048 + 1 * 512>(vbase); \
        const s16x4 c0 = tr_read<(2 * (ks)) * 2048 + 2 * 512>(vbase), c1 = tr_read<(2 * (ks) + 1) * 2048 + 2 * 512>(vbase); \
        const s16x4 e0 = tr_read<(2 * (ks)) * 2048 + 3 * 512>(vbase), e1 = tr_read<(2 * (ks) + 1) * 2048 + 3 * 512>(vbase); \
        asm volatile("s_waitcnt lgkmcnt(0)" ::: "memory"); __builtin_amdgcn_sched_barrier(0); \
        o[0] = MFMA(__builtin_shufflevector(a0, a1, 0, 1, 2, 3, 4, 5, 6, 7), pa[ks], o[0]); \
        o[1] = MFMA(__builtin_shufflevector(b0, b1, 0, 1, 2, 3, 4, 5, 6, 7), pa[ks], o[1]); \
        o[2] = MFMA(__builtin_shufflevector(c0, c1, 0, 1, 2, 3, 4, 5, 6, 7), pa[ks], o[2]); \
        o[3] = MFMA(__builtin_shufflevector(e0, e1, 0, 1, 2, 3, 4, 5, 6, 7), pa[ks], o[3]); \
      } while (0)
      PVSTEP(0); PVSTEP(1); PVSTEP(2); PVSTEP(3); PVSTEP(4); PVSTEP(5); PVSTEP(6); PVSTEP(7);
      PVSTEP(8); PVSTEP(9); PVSTEP(10); PVSTEP(11); PVSTEP(12); PVSTEP(13); PVSTEP(14); PVSTEP(15);
#undef PVSTEP
      char* Ow = reinterpret_cast<char*>(O + (row0 + r32) * D) + hi * 16;
#pragma unroll
      for (int d0 = 0; d0 < 4; ++d0) {
#pragma unroll
        for (int gp = 0; gp < 2; ++gp) {
          const int ga = 2 * gp, gb = 2 * gp + 1;
          unsigned ax = cvtpk(o[d0][4 * ga + 0] * rl, o[d0][4 * ga + 1] * rl), ay = cvtpk(o[d0][4 * ga + 2] * rl, o[d0][4 * ga + 3] * rl);
          unsigned bx = cvtpk(o[d0][4 * gb + 0] * rl, o[d0][4 * gb + 1] * rl), by = cvtpk(o[d0][4 * gb + 2] * rl, o[d0][4 * gb + 3] * rl);
          auto r0 = __builtin_amdgcn_permlane32_swap(ax, bx, false, false);
          auto r1 = __builtin_amdgcn_permlane32_swap(ay, by, false, false);
          u32x4 wv = {r0[0], r1[0], r0[1], r1[1]};
          *reinterpret_cast<u32x4*>(Ow + d0 * 64 + gp * 32) = wv;
        }
      }
    }
    __syncthreads();
  }
}

extern "C" void kernel_launch(void* const* d_in, const int* in_sizes, int n_in,
                              void* d_out, int out_size, void* d_ws, size_t ws_size,
                              hipStream_t stream) {
  (void)in_sizes; (void)n_in; (void)out_size; (void)d_ws; (void)ws_size;
  hipFuncSetAttribute((const void*)gqa_attn_kernel, hipFuncAttributeMaxDynamicSharedMemorySize, LDS_BYTES);
  hipLaunchKernelGGL(gqa_attn_kernel, dim3(256), dim3(512), LDS_BYTES, stream,
                     (const bf16*)d_in[0], (const bf16*)d_in[1], (const bf16*)d_in[2], (bf16*)d_out);
}
```

```cpp
#include <hip/hip_runtime.h>
#include <hip/hip_bf16.h>

using bf16 = __hip_bfloat16;
using bf16x8 = __attribute__((ext_vector_type(8))) short;
using s16x4  = __attribute__((ext_vector_type(4))) short;
using f32x16 = __attribute__((ext_vector_type(16))) float;
using u32x4  = __attribute__((ext_vector_type(4))) unsigned;
typedef __attribute__((address_space(3))) s16x4* lds_s16x4p;
typedef __attribute__((address_space(3))) unsigned* lds_u32p;

constexpr int B_ = 64, HQ = 32, HKV = 8, SQ = 256, SK = 256, D = 128;
constexpr int NGRP = B_ * HKV;
constexpr int GROWS = (HQ / HKV) * SQ;
constexpr int LDS_BYTES = 2 * SK * D * 2;
constexpr int VOFF = SK * D * 2;

#define MFMA(a, b, c) __builtin_amdgcn_mfma_f32_32x32x16_bf16((a), (b), (c), 0, 0, 0)

__device__ __forceinline__ unsigned cvtpk(float lo, float hi) {
  unsigned r; asm volatile("v_cvt_pk_bf16_f32 %0, %1, %2" : "=v"(r) : "v"(lo), "v"(hi)); return r;
}

template <int OFF> __device__ __forceinline__ s16x4 tr_read(int a) {
  s16x4 r; asm volatile("ds_read_b64_tr_b16 %0, %1 offset:%2" : "=v"(r) : "v"(a), "i"(OFF) : "memory"); return r;
}
template <int OFF> __device__ __forceinline__ bf16x8 ds_read128(int a) {
  bf16x8 r; asm volatile("ds_read_b128 %0, %1 offset:%2" : "=v"(r) : "v"(a), "i"(OFF) : "memory"); return r;
}
__device__ __forceinline__ void dma_k(const bf16* __restrict__ Kg, char* lds, int w, int l) {
  asm volatile("" : "+v"(l));
#pragma unroll
  for (int i = 0; i < 8; ++i) {
    const int j = w * 8 + i, kb = j >> 1;
    const int ch = ((j & 1) * 8 + (l >> 3)) ^ ((kb >> 1) & 1);
    __builtin_amdgcn_global_load_lds((const unsigned*)(Kg + (kb * 8 + (l & 7)) * D + ch * 8), (lds_u32p)(lds + j * 1024), 16, 0, 0);
  }
}
__device__ __forceinline__ void dma_v(const bf16* __restrict__ Vg, char* lds, int w, int l) {
  asm volatile("" : "+v"(l));
#pragma unroll
  for (int i = 0; i < 8; ++i) {
    const int j = w * 8 + i, kb = j >> 1, cb = (j & 1) * 2 + (l >> 5), row = (l >> 2) & 7, c8 = l & 3;
    __builtin_amdgcn_global_load_lds((const unsigned*)(Vg + (kb * 8 + row) * D + cb * 32 + c8 * 8), (lds_u32p)(lds + VOFF + j * 1024), 16, 0, 0);
  }
}

__global__ __launch_bounds__(512, 1)
void gqa_attn_kernel(const bf16* __restrict__ Q, const bf16* __restrict__ K, const bf16* __restrict__ V, bf16* __restrict__ O) {
  extern __shared__ __attribute__((aligned(16))) char lds[];
  const int tid = threadIdx.x, w = tid >> 6, l = tid & 63, r32 = l & 31, hi = l >> 5;
  constexpr float C = 0.088388347648318440f * 1.4426950408889634f;
  const int kbase = (r32 >> 3) * 2048 + (hi ^ (r32 >> 4)) * 128 + (r32 & 7) * 16;
  int vbase = VOFF + (4 * hi + ((l >> 2) & 3)) * 64 + ((l >> 4) & 1) * 32 + (l & 3) * 8;

  const int kaddr = (int)(uintptr_t)lds + kbase, vaddr = (int)(uintptr_t)lds + vbase;
  int g = blockIdx.x;
  dma_k(K + (size_t)g * SK * D, lds, w, l);
  dma_v(V + (size_t)g * SK * D, lds, w, l);
  bf16x8 q[8];
  {
    const bf16* Qw = Q + ((size_t)g * GROWS + w * 32 + r32) * D + hi * 8;
#pragma unroll
    for (int d0 = 0; d0 < 8; ++d0) q[d0] = *reinterpret_cast<const bf16x8*>(Qw + d0 * 16);
  }
  asm volatile("s_waitcnt vmcnt(0)" ::: "memory");
  __syncthreads();

  for (; g < NGRP; g += gridDim.x) {
    const int gn = g + gridDim.x;
    const bool has_next = gn < NGRP;
    for (int it = 0; it < 4; ++it) {
      const size_t row0 = (size_t)g * GROWS + (it * 8 + w) * 32;
      asm volatile("s_waitcnt vmcnt(8)" : "+v"(q[0]), "+v"(q[1]), "+v"(q[2]), "+v"(q[3]), "+v"(q[4]), "+v"(q[5]), "+v"(q[6]), "+v"(q[7]) :: "memory");
      f32x16 s[8];
      {
        bf16x8 kf[8];
#define KRD(i) kf[(i) & 7] = ds_read128<((i) & 7) * 256 + ((i) >> 3) * 8192>(kaddr)
        KRD(0); KRD(1); KRD(2); KRD(3); KRD(4); KRD(5); KRD(6);
#define QK1(i) do { if ((i) + 7 < 64) { KRD((i) + 7); asm volatile("s_waitcnt lgkmcnt(7)" ::: "memory"); } \
          else asm volatile("s_waitcnt lgkmcnt(%0)" :: "i"(63 - (i)) : "memory"); \
          __builtin_amdgcn_sched_barrier(0); \
          s[(i) >> 3] = MFMA(kf[(i) & 7], q[(i) & 7], ((i) & 7) ? s[(i) >> 3] : f32x16{}); \
          __builtin_amdgcn_sched_barrier(0); } while (0)
#define QK8(i) QK1(i); QK1(i + 1); QK1(i + 2); QK1(i + 3); QK1(i + 4); QK1(i + 5); QK1(i + 6); QK1(i + 7)
        QK8(0); QK8(8); QK8(16); QK8(24); QK8(32); QK8(40); QK8(48); QK8(56);
      }
      {
        const size_t nrow = (it < 3) ? row0 + 256 : (size_t)gn * GROWS + w * 32;
        if (it < 3 || has_next) {
          const bf16* Qw = Q + (nrow + r32) * D + hi * 8;
#pragma unroll
          for (int d0 = 0; d0 < 8; ++d0) asm volatile("global_load_dwordx4 %0, %1, off offset:%2" : "=v"(q[d0]) : "v"(Qw), "i"(d0 * 32) : "memory");
        }
      }
      if (it == 3 && has_next) {
        __syncthreads();
        dma_k(K + (size_t)gn * SK * D, lds, w, l);
      }
      float m = s[0][0];
#pragma unroll
      for (int kt = 0; kt < 8; ++kt)
#pragma unroll
        for (int r = 0; r < 16; ++r) m = fmaxf(m, s[kt][r]);
      { auto rr = __builtin_amdgcn_permlane32_swap(__float_as_uint(m), __float_as_uint(m), false, false);
        m = fmaxf(__uint_as_float(rr[0]), __uint_as_float(rr[1])); }
      const float mC = -m * C;
      float lsum = 0.f;
      bf16x8 pa[16];
#pragma unroll
      for (int kt = 0; kt < 8; ++kt) {
#pragma unroll
        for (int r = 0; r < 16; ++r) { float p = __builtin_amdgcn_exp2f(fmaf(s[kt][r], C, mC)); s[kt][r] = p; lsum += p; }
#pragma unroll
        for (int h2 = 0; h2 < 2; ++h2) {
          u32x4 wv = {cvtpk(s[kt][8 * h2 + 0], s[kt][8 * h2 + 1]), cvtpk(s[kt][8 * h2 + 2], s[kt][8 * h2 + 3]),
                      cvtpk(s[kt][8 * h2 + 4], s[kt][8 * h2 + 5]), cvtpk(s[kt][8 * h2 + 6], s[kt][8 * h2 + 7])};
          pa[kt * 2 + h2] = __builtin_bit_cast(bf16x8, wv);
        }
      }
      { auto rr = __builtin_amdgcn_permlane32_swap(__float_as_uint(lsum), __float_as_uint(lsum), false, false);
        lsum = __uint_as_float(rr[0]) + __uint_as_float(rr[1]); }
      const float rl = __builtin_amdgcn_rcpf(lsum);
      if (it == 0 && g != (int)blockIdx.x) {
        asm volatile("s_waitcnt vmcnt(0)" ::: "memory");
        __syncthreads();
      }
      __builtin_amdgcn_sched_barrier(0);
      f32x16 o[4];
      {
        s16x4 va[6], vb[6];
#define VRD(i) do { va[(i) % 6] = tr_read<(2 * ((i) >> 2)) * 2048 + ((i) & 3) * 512>(vaddr); vb[(i) % 6] = tr_read<(2 * ((i) >> 2) + 1) * 2048 + ((i) & 3) * 512>(vaddr); } while (0)
        VRD(0); VRD(1); VRD(2); VRD(3); VRD(4);
#define PV1(i) do { if ((i) + 5 < 64) { VRD((i) + 5); asm volatile("s_waitcnt lgkmcnt(10)" ::: "memory"); } \
          else asm volatile("s_waitcnt lgkmcnt(%0)" :: "i"(2 * (63 - (i))) : "memory"); \
          __builtin_amdgcn_sched_barrier(0); \
          o[(i) & 3] = MFMA(__builtin_shufflevector(va[(i) % 6], vb[(i) % 6], 0, 1, 2, 3, 4, 5, 6, 7), pa[(i) >> 2], ((i) >> 2) ? o[(i) & 3] : f32x16{}); \
          __builtin_amdgcn_sched_barrier(0); } while (0)
#define PV8(i) PV1(i); PV1(i + 1); PV1(i + 2); PV1(i + 3); PV1(i + 4); PV1(i + 5); PV1(i + 6); PV1(i + 7)
        PV8(0); PV8(8); PV8(16); PV8(24); PV8(32); PV8(40); PV8(48); PV8(56);
      }
      char* Ow = reinterpret_cast<char*>(O + (row0 + r32) * D) + hi * 16;
#pragma unroll
      for (int d0 = 0; d0 < 4; ++d0) {
#pragma unroll
        for (int gp = 0; gp < 2; ++gp) {
          const int ga = 2 * gp, gb = 2 * gp + 1;
          unsigned ax = cvtpk(o[d0][4 * ga + 0] * rl, o[d0][4 * ga + 1] * rl), ay = cvtpk(o[d0][4 * ga + 2] * rl, o[d0][4 * ga + 3] * rl);
          unsigned bx = cvtpk(o[d0][4 * gb + 0] * rl, o[d0][4 * gb + 1] * rl), by = cvtpk(o[d0][4 * gb + 2] * rl, o[d0][4 * gb + 3] * rl);
          auto r0 = __builtin_amdgcn_permlane32_swap(ax, bx, false, false);
          auto r1 = __builtin_amdgcn_permlane32_swap(ay, by, false, false);
          u32x4 wv = {r0[0], r1[0], r0[1], r1[1]};
          *reinterpret_cast<u32x4*>(Ow + d0 * 64 + gp * 32) = wv;
        }
      }
      if (it == 3 && has_next) {
        asm volatile("s_waitcnt vmcnt(0)" ::: "memory");
        __syncthreads();
        dma_v(V + (size_t)gn * SK * D, lds, w, l);
      }
    }
  }
}

extern "C" void kernel_launch(void* const* d_in, const int* in_sizes, int n_in,
                              void* d_out, int out_size, void* d_ws, size_t ws_size,
                              hipStream_t stream) {
  (void)in_sizes; (void)n_in; (void)out_size; (void)d_ws; (void)ws_size;
  (void)hipFuncSetAttribute((const void*)gqa_attn_kernel, hipFuncAttributeMaxDynamicSharedMemorySize, LDS_BYTES);
  hipLaunchKernelGGL(gqa_attn_kernel, dim3(256), dim3(512), LDS_BYTES, stream,
                     (const bf16*)d_in[0], (const bf16*)d_in[1], (const bf16*)d_in[2], (bf16*)d_out);
}
```
